```python
import torch

def _softmax(x, axis=-1):
    return torch.softmax(x, dim=axis)
def _gelu(x):
    return torch.nn.functional.gelu(x, approximate="tanh")
def _silu(x):
    return torch.nn.functional.silu(x)
def _sigmoid(x):
    return torch.sigmoid(x)
def _layernorm(x, axis=-1, eps=1e-5):
    m = x.mean(dim=axis, keepdim=True)
    v = x.var(dim=axis, keepdim=True, unbiased=False)
    return (x - m) / torch.sqrt(v + eps)
def _scatter_add(tgt, idx, src, axis=0):
    return tgt.clone().index_add_(axis, idx, src)
def _segment_sum(src, seg, n):
    out = torch.zeros((n,) + src.shape[1:], dtype=src.dtype)
    return out.index_add_(0, seg, src)
def _pad(x, pw):
    flat = tuple(v for pair in reversed(pw) for v in pair)
    return torch.nn.functional.pad(x, flat)

kernel_name = 'standalone__moe_arch_mod__prompt'

def setup_inputs():

    _w = (lambda a: (lambda t: t.to(torch.bfloat16) if t.is_floating_point() else t)(torch.as_tensor(a)))
    B, S, D, F, C = 1, 2048, 576, 4608, 2
    return {
        "x": _w((lambda *s: torch.randn(*s, dtype=torch.bfloat16) * 0.1)(B, S, D)),
        "w_r": _w((lambda *s: torch.randn(*s, dtype=torch.bfloat16) * 0.1)(D)),
        "w_gate": _w((lambda *s: torch.randn(*s, dtype=torch.bfloat16) * 0.1)(D, F)),
        "w_up": _w((lambda *s: torch.randn(*s, dtype=torch.bfloat16) * 0.1)(D, F)),
        "w_down": _w((lambda *s: torch.randn(*s, dtype=torch.bfloat16) * 0.1)(F, D)),
    }

def reference(x, w_r, w_gate, w_up, w_down):
    B, S, D = x.shape
    C = 2
    r = x @ w_r
    idx = torch.argsort(-r, axis=-1)[:, :C]
    idx_e = (lambda x, n, axis: torch.repeat_interleave(x, n, dim=axis))(idx.reshape(B, C, 1), D, axis=2)
    x_sel = (lambda x, i, axis: torch.take_along_dim(x, i, dim=axis))(x, idx_e, axis=1)
    g = _silu(x_sel @ w_gate)
    u = x_sel @ w_up
    y = (g * u) @ w_down
    r_sel = _sigmoid((lambda x, i, axis: torch.take_along_dim(x, i, dim=axis))(r, idx, axis=1))
    y = y * r_sel.reshape(B, C, 1)
    b_off = (torch.arange(B) * S).reshape(B, 1)
    flat_idx = (idx + b_off).reshape(B * C)
    zero = (x * 0.0).reshape(B * S, D)
    upd = _scatter_add(zero, flat_idx, y.reshape(B * C, D), axis=0)
    return x + upd.reshape(B, S, D)
```

```cpp
#include <hip/hip_runtime.h>
#include <stdint.h>

#define GAS __attribute__((address_space(1)))
#define LAS __attribute__((address_space(3)))
typedef unsigned v4u __attribute__((ext_vector_type(4)));
typedef unsigned v2u __attribute__((ext_vector_type(2)));
typedef float f32x4 __attribute__((ext_vector_type(4)));
typedef __bf16 bf16x8 __attribute__((ext_vector_type(8)));
typedef __bf16 bf16x2 __attribute__((ext_vector_type(2)));
typedef short s16x4 __attribute__((ext_vector_type(4)));
typedef unsigned long long u64;

namespace moe {
constexpr int S = 2048, D = 576, F = 4608;
constexpr int NDN = 72;
constexpr int NROUT = 64;
constexpr int NGU = 144;
constexpr int NRED = 36;
constexpr int NWG = NDN + NGU + NRED;
constexpr int ROWB = D * 2;
constexpr int FROWB = F * 2;
constexpr int WS_EPOCH = 0;
constexpr int WS_CAND = 4096;
constexpr int WS_AU = 8192;
constexpr int WS_PART = 65536;
constexpr int WS_BYTES = WS_PART + NRED * NDN * 128;
constexpr int WS_STAMP = 1 << 20;
constexpr int L_W = 0, L_X = 73728, L_WR = L_X + 2304, L_KEY = L_WR + 4 * 1280, L_HS = L_KEY + 128, L_RED = L_HS + 1024, L_BYTES = 84 * 1024;
static_assert(L_RED + 512 <= L_BYTES, "LDS map");
constexpr unsigned SENT = 0xFFFFFFFFu;
constexpr unsigned SPIN_CAP = 1u << 18;
constexpr int AUX_SC1 = 16;

#define MOE_WAUX 2
#define MOE_DAUX MOE_WAUX
#define MOE_OAUX 16

#define STAMP(i) do {} while (0)
#define PROBE_END() do {} while (0)

__device__ __forceinline__ float as_f(unsigned u) { return __builtin_bit_cast(float, u); }
__device__ __forceinline__ unsigned as_u(float f) { return __builtin_bit_cast(unsigned, f); }
__device__ __forceinline__ float dot2(unsigned a, unsigned b, float c) {
  return __builtin_amdgcn_fdot2_f32_bf16(__builtin_bit_cast(bf16x2, a), __builtin_bit_cast(bf16x2, b), c, false);
}
__device__ __forceinline__ float bflo(unsigned u) { return as_f(u << 16); }
__device__ __forceinline__ float bfhi(unsigned u) { return as_f(u & 0xffff0000u); }
__device__ __forceinline__ unsigned f2bf(float f) { return (unsigned)__builtin_bit_cast(unsigned short, (__bf16)f); }
__device__ __forceinline__ float bfr(float f) { return as_f(f2bf(f) << 16); }
__device__ __forceinline__ unsigned pack2(float lo, float hi) {
  bf16x2 v; v[0] = (__bf16)lo; v[1] = (__bf16)hi; return __builtin_bit_cast(unsigned, v);
}
__device__ __forceinline__ float sigmoidf_(float a) { return __builtin_amdgcn_rcpf(1.0f + __expf(-a)); }
__device__ __forceinline__ bf16x8 frag(s16x4 a, s16x4 b) {
  return __builtin_bit_cast(bf16x8, __builtin_shufflevector(a, b, 0, 1, 2, 3, 4, 5, 6, 7));
}
__device__ __forceinline__ bf16x8 frag(v4u a) { return __builtin_bit_cast(bf16x8, a); }
__device__ __forceinline__ s16x4 trread(LAS unsigned char* p) { return __builtin_amdgcn_ds_read_tr16_b64_v4i16((LAS s16x4*)p); }
__device__ __forceinline__ unsigned mkkey(unsigned rb, int row) {
  unsigned u = (rb & 0x7fffu) ? rb : 0u;
  unsigned m = (u & 0x8000u) ? (~u & 0xffffu) : (u | 0x8000u);
  m = ((u & 0x7fffu) > 0x7f80u) ? 0u : m;
  return (m << 11) | (unsigned)(2047 - row);
}
__device__ __forceinline__ int key_row(unsigned k) { return 2047 - (int)(k & 2047u); }
__device__ __forceinline__ float key_r(unsigned k) {
  unsigned m = k >> 11;
  unsigned rb = (m & 0x8000u) ? (m & 0x7fffu) : (m ? (~m & 0xffffu) : 0x7fc0u);
  return as_f(rb << 16);
}
template <int CTRL, int RM> __device__ __forceinline__ unsigned dppu(unsigned v) {
  return (unsigned)__builtin_amdgcn_update_dpp(0, (int)v, CTRL, RM, 0xf, true);
}
template <int CTRL, int RM> __device__ __forceinline__ float dppf(float v) { return as_f(dppu<CTRL, RM>(as_u(v))); }
template <int CTRL, int RM> __device__ __forceinline__ void top2_step(unsigned& k0, unsigned& k1) {
  const unsigned o0 = dppu<CTRL, RM>(k0), o1 = dppu<CTRL, RM>(k1);
  const unsigned lo = min(k0, o0);
  k0 = max(k0, o0);
  k1 = max(max(k1, o1), lo);
}
__device__ __forceinline__ void wave_top2(unsigned& k0, unsigned& k1) {
  top2_step<0x111, 0xf>(k0, k1);
  top2_step<0x112, 0xf>(k0, k1);
  top2_step<0x114, 0xf>(k0, k1);
  top2_step<0x118, 0xf>(k0, k1);
  top2_step<0x142, 0xa>(k0, k1);
  top2_step<0x143, 0xc>(k0, k1);
  k0 = (unsigned)__builtin_amdgcn_readlane((int)k0, 63);
  k1 = (unsigned)__builtin_amdgcn_readlane((int)k1, 63);
}
__device__ __forceinline__ v4u ld_slot_a(__amdgpu_buffer_rsrc_t rs, int lane) {
  return __builtin_amdgcn_raw_buffer_load_b128(rs, WS_CAND + lane * 16, 0, AUX_SC1);
}
__device__ __forceinline__ v4u ld_slot_b(__amdgpu_buffer_rsrc_t rs, int lane) {
  return __builtin_amdgcn_raw_buffer_load_b128(rs, WS_CAND + 1024 + (lane < NDN + NRED - 64 ? lane : 0) * 16, 0, AUX_SC1);
}
__device__ __forceinline__ void gather_top2(__amdgpu_buffer_rsrc_t rs, unsigned tlo, unsigned thi, int lane, v4u c, v4u d,
                                            unsigned& k0, unsigned& k1) {
  for (unsigned spins = 0;; ++spins) {
    const bool ok = (c.x == tlo) & ((c.y & 31u) == thi) & (c.z == tlo) & ((c.w & 31u) == thi) &
                    (d.x == tlo) & ((d.y & 31u) == thi) & (d.z == tlo) & ((d.w & 31u) == thi);
    if (__all(ok) || spins > SPIN_CAP) break;
    asm volatile("" ::: "memory");
    c = ld_slot_a(rs, lane);
    d = ld_slot_b(rs, lane);
  }
  k0 = c.y >> 5; k1 = c.w >> 5;
  wave_top2(k0, k1);
}

__global__ void __launch_bounds__(256, 1)
moe_kernel(const unsigned char* __restrict__ x, const unsigned char* __restrict__ wr, const unsigned char* __restrict__ wg,
           const unsigned char* __restrict__ wu, const unsigned char* __restrict__ wd, unsigned char* out, unsigned char* ws) {
  extern __shared__ __attribute__((aligned(16))) unsigned char smem[];
  LAS unsigned char* L = (LAS unsigned char*)smem;
  const int tid = threadIdx.x, lane = tid & 63, w = __builtin_amdgcn_readfirstlane(tid >> 6), b = blockIdx.x;
  const __amdgpu_buffer_rsrc_t rs = __builtin_amdgcn_make_buffer_rsrc((void*)ws, 0, WS_BYTES, 0x00020000);
  STAMP(0);
  const v2u ev = __builtin_amdgcn_raw_buffer_load_b64(rs, WS_EPOCH, 0, AUX_SC1);
  const int g = lane >> 4, q4 = (lane >> 2) & 3, p = lane & 3, jp = lane & 15;

  if (b >= NDN && b < NDN + NGU) {
    const int gam = b - NDN, jg = gam >> 1, m = gam & 1;
    {
      const int s = (((lane >> 4) & 1) << 1) | ((w & 1) << 2);
      const GAS unsigned char* g0 = (const GAS unsigned char*)(m ? wu : wg) + jg * 128 + (lane >> 3) * FROWB + ((lane & 7) ^ s) * 16;
#pragma unroll
      for (int i = 0; i < 18; ++i) {
        const int q = w + 4 * i;
        __builtin_amdgcn_global_load_lds((const GAS void*)(g0 + (size_t)q * (8 * FROWB)), (LAS void*)(L + L_W + q * 1024), 16, 0, MOE_WAUX);
      }
    }
    v4u c = {0u, 0u, 0u, 0u}, dd = {0u, 0u, 0u, 0u};
    if (w == 0) { c = ld_slot_a(rs, lane); dd = ld_slot_b(rs, lane); }
    asm volatile("s_waitcnt vmcnt(0)" ::: "memory");
    __builtin_amdgcn_s_barrier();
    STAMP(1);
    if (w == 0) {
      const u64 tag = ((((u64)ev.y << 32) | ev.x) + 1ull);
      const unsigned tlo = __builtin_amdgcn_readfirstlane((unsigned)tag), thi = __builtin_amdgcn_readfirstlane((unsigned)(tag >> 32) & 31u);
      unsigned k0, k1;
      gather_top2(rs, tlo, thi, lane, c, dd, k0, k1);
      STAMP(2);
      const GAS unsigned char* x0 = (const GAS unsigned char*)x + (size_t)key_row(k0) * ROWB;
      const GAS unsigned char* x1 = (const GAS unsigned char*)x + (size_t)key_row(k1) * ROWB;
      __builtin_amdgcn_global_load_lds((const GAS void*)(x0 + lane * 16), (LAS void*)(L + L_X), 16, 0, 0);
      __builtin_amdgcn_global_load_lds((const GAS void*)(lane < 8 ? x0 + 1024 + lane * 16 : x1 + (lane - 8) * 16), (LAS void*)(L + L_X + 1024), 16, 0, 0);
      if (lane < 16) __builtin_amdgcn_global_load_lds((const GAS void*)(x1 + 896 + lane * 16), (LAS void*)(L + L_X + 2048), 16, 0, 0);
    }
    s16x4 a[18][2];
    {
      const int s = (((q4 >> 1) & 1) << 1) | ((g & 1) << 2);
      LAS unsigned char* base = L + L_W + (8 * g + q4) * 128 + (((2 * w + (p >> 1)) ^ s) * 16) + 8 * (p & 1);
#pragma unroll
      for (int ks = 0; ks < 18; ++ks) { a[ks][0] = trread(base + ks * 4096); a[ks][1] = trread(base + ks * 4096 + 512); }
    }
    asm volatile("s_waitcnt vmcnt(0) lgkmcnt(0)" ::: "memory");
    __builtin_amdgcn_s_barrier();
    STAMP(3);
    f32x4 acc = {0.f, 0.f, 0.f, 0.f};
    {
      LAS unsigned char* xb = L + L_X + (jp & 1) * ROWB + g * 16;
#pragma unroll
      for (int ks = 0; ks < 18; ++ks)
        acc = __builtin_amdgcn_mfma_f32_16x16x32_bf16(frag(a[ks][0], a[ks][1]), frag(*(const LAS v4u*)(xb + ks * 64)), acc, 0, 0, 0);
    }
    v4u o;
    {
      const float a0 = acc[0], a1 = acc[1], a2 = acc[2], a3 = acc[3];
      o.x = min(pack2(a0, dppf<0xB1, 0xf>(a0)), SENT - 1u);
      o.y = min(pack2(a1, dppf<0xB1, 0xf>(a1)), SENT - 1u);
      o.z = min(pack2(a2, dppf<0xB1, 0xf>(a2)), SENT - 1u);
      o.w = min(pack2(a3, dppf<0xB1, 0xf>(a3)), SENT - 1u);
    }
    if (jp == 0) __builtin_amdgcn_raw_buffer_store_b128(o, rs, WS_AU + jg * 512 + m * 256 + (16 * w + 4 * g) * 4, 0, AUX_SC1);
    STAMP(4);
    return;
  }

  if (b >= NDN + NGU) {
    const int rho = b - (NDN + NGU), t = rho / 18, n0 = (rho % 18) * 32;
    const int ib = WS_PART + rho * NDN * 128;
    {
      const v4u s = {SENT, SENT, SENT, SENT};
#pragma unroll
      for (int i = 0; i < 3; ++i)
        if (i < 2 || tid < 64) __builtin_amdgcn_raw_buffer_store_b128(s, rs, ib + (tid + 256 * i) * 16, 0, AUX_SC1);
    }
    asm volatile("s_waitcnt vmcnt(0)" ::: "memory");
    __builtin_amdgcn_s_barrier();
    const u64 tag = ((((u64)ev.y << 32) | ev.x) + 1ull);
    const unsigned tlo = __builtin_amdgcn_readfirstlane((unsigned)tag), thi = __builtin_amdgcn_readfirstlane((unsigned)(tag >> 32) & 31u);
    if (tid == 0) __builtin_amdgcn_raw_buffer_store_b128(v4u{tlo, thi, tlo, thi}, rs, WS_CAND + (NDN + rho) * 16, 0, AUX_SC1);
    STAMP(1);
    float sig = 0.f, xr = 0.f; int row = 0;
    if (w == 0) {
      unsigned k0, k1;
      gather_top2(rs, tlo, thi, lane, ld_slot_a(rs, lane), ld_slot_b(rs, lane), k0, k1);
      const unsigned k = t ? k1 : k0;
      row = key_row(k);
      sig = bfr(sigmoidf_(key_r(k)));
      xr = bflo(*(const GAS unsigned short*)((const GAS unsigned char*)x + (size_t)row * ROWB + (n0 + (lane & 31)) * 2));
    }
    STAMP(2);
    f32x4 acc;
    for (unsigned spins = 0;; ++spins) {
      v4u v[3];
      asm volatile("" ::: "memory");
#pragma unroll
      for (int i = 0; i < 3; ++i) {
        v[i] = v4u{0u, 0u, 0u, 0u};
        if (i < 2 || w == 0) v[i] = __builtin_amdgcn_raw_buffer_load_b128(rs, ib + (tid + 256 * i) * 16, 0, AUX_SC1);
      }
      bool ok = true;
      acc = f32x4{0.f, 0.f, 0.f, 0.f};
#pragma unroll
      for (int i = 0; i < 3; ++i) {
        ok &= (v[i].x != SENT) & (v[i].y != SENT) & (v[i].z != SENT) & (v[i].w != SENT);
        acc += __builtin_bit_cast(f32x4, v[i]);
      }
      if (__all(ok) || spins > SPIN_CAP) break;
    }
    STAMP(3);
    {
      const auto s0 = __builtin_amdgcn_permlane32_swap(as_u(acc[0]), as_u(acc[1]), false, false);
      const auto s1 = __builtin_amdgcn_permlane32_swap(as_u(acc[2]), as_u(acc[3]), false, false);
      const float p01 = as_f(s0[0]) + as_f(s0[1]);
      const float p23 = as_f(s1[0]) + as_f(s1[1]);
      const auto s2 = __builtin_amdgcn_permlane16_swap(as_u(p01), as_u(p23), false, false);
      float sum = as_f(s2[0]) + as_f(s2[1]);
      sum += dppf<0x128, 0xf>(sum);
      const int e = ((g & 1) << 1) | (g >> 1);
      if ((lane & 8) == 0) ((LAS float*)(L + L_RED))[w * 32 + (lane & 7) * 4 + e] = sum;
    }
    asm volatile("s_waitcnt lgkmcnt(0)" ::: "memory");
    __builtin_amdgcn_s_barrier();
    if (w == 0) {
      const LAS float* red = (const LAS float*)(L + L_RED);
      const int o = lane & 31;
      float y = (red[o] + red[32 + o]) + (red[64 + o] + red[96 + o]);
      y = bfr(bfr(y) * sig);
      const float upd = bfr(__builtin_fmaf(xr, 0.0f, y));
      const unsigned ob = f2bf(xr + upd);
      if (lane < 32) *(GAS unsigned short*)((GAS unsigned char*)out + (size_t)row * ROWB + (n0 + o) * 2) = (unsigned short)ob;
      if (rho == 0 && lane == 0) {
        const u64 e1 = ((((u64)ev.y << 32) | ev.x) + 1ull);
        __builtin_amdgcn_raw_buffer_store_b64(v2u{(unsigned)e1, (unsigned)(e1 >> 32)}, rs, WS_EPOCH, 0, AUX_SC1);
      }
    }
    STAMP(4);
    PROBE_END();
    return;
  }

  {
    const bool routes = b < NROUT;
    if (tid < 32) __builtin_amdgcn_raw_buffer_store_b128(v4u{SENT, SENT, SENT, SENT}, rs, WS_AU + b * 512 + tid * 16, 0, AUX_SC1);
    const int rl = w * 8 + (lane >> 3), row = (routes ? b * 32 : 0) + rl, jj = lane & 7;
    v4u xv[9];
    unsigned key = 0u, k0 = 0u, k1 = 0u;
    if (routes) {
      const GAS unsigned char* wrp = (const GAS unsigned char*)wr;
      __builtin_amdgcn_global_load_lds((const GAS void*)(wrp + lane * 16), (LAS void*)(L + L_WR + w * 1280), 16, 0, 0);
      if (lane < 8) __builtin_amdgcn_global_load_lds((const GAS void*)(wrp + 1024 + lane * 16), (LAS void*)(L + L_WR + w * 1280 + 1024), 16, 0, 0);
      __builtin_amdgcn_sched_barrier(0);
      const GAS v4u* xp = (const GAS v4u*)((const GAS unsigned char*)x + (size_t)row * ROWB) + jj;
#pragma unroll
      for (int i = 0; i < 9; ++i) xv[i] = xp[8 * i];
      __builtin_amdgcn_sched_barrier(0);
      asm volatile("s_waitcnt vmcnt(9)" ::: "memory");
      v4u wv[9];
#pragma unroll
      for (int i = 0; i < 9; ++i) wv[i] = *(const LAS v4u*)(L + L_WR + w * 1280 + (jj + 8 * i) * 16);
      float s0 = 0.f, s1 = 0.f, s2 = 0.f, s3 = 0.f;
#pragma unroll
      for (int i = 0; i < 9; ++i) {
        s0 = dot2(xv[i].x, wv[i].x, s0); s1 = dot2(xv[i].y, wv[i].y, s1);
        s2 = dot2(xv[i].z, wv[i].z, s2); s3 = dot2(xv[i].w, wv[i].w, s3);
      }
      float r = (s0 + s1) + (s2 + s3);
      r += dppf<0xB1, 0xf>(r);
      r += dppf<0x4E, 0xf>(r);
      r += dppf<0x141, 0xf>(r);
      key = mkkey(f2bf(r), row);
      LAS unsigned* keys = (LAS unsigned*)(L + L_KEY);
      if (jj == 0) keys[rl] = key;
      asm volatile("s_waitcnt vmcnt(0) lgkmcnt(0)" ::: "memory");
      __builtin_amdgcn_s_barrier();
      if (w == 0) {
        k0 = lane < 32 ? keys[lane & 31] : 0u;
        wave_top2(k0, k1);
      }
    } else {
#pragma unroll
      for (int i = 0; i < 9; ++i) xv[i] = v4u{0u, 0u, 0u, 0u};
      asm volatile("s_waitcnt vmcnt(0)" ::: "memory");
    }
    const u64 tag = ((((u64)ev.y << 32) | ev.x) + 1ull);
    const unsigned tlo = __builtin_amdgcn_readfirstlane((unsigned)tag), thi = __builtin_amdgcn_readfirstlane((unsigned)(tag >> 32) & 31u);
    if (tid == 0) __builtin_amdgcn_raw_buffer_store_b128(v4u{tlo, (k0 << 5) | thi, tlo, (k1 << 5) | thi}, rs, WS_CAND + b * 16, 0, AUX_SC1);
    STAMP(1);
    {
      const GAS unsigned char* d0 = (const GAS unsigned char*)wd + (size_t)(64 * b + 16 * w) * ROWB;
#pragma unroll
      for (int i = 0; i < 18; ++i) {
        const int fi = (64 * i) / 72, thr = 72 * (fi + 1) - 64 * i;
        const unsigned f = fi + (lane >= thr ? 1 : 0);
        const unsigned off = (unsigned)((64 * i + lane) * 16) ^ ((f & 3u) << 4);
        __builtin_amdgcn_global_load_lds((const GAS void*)(d0 + off), (LAS void*)(L + L_W + (18 * w + i) * 1024), 16, 0, MOE_DAUX);
      }
    }
    v4u c = ld_slot_a(rs, lane), dd = ld_slot_b(rs, lane);
    asm volatile("s_waitcnt vmcnt(0)" ::: "memory");
    __builtin_amdgcn_s_barrier();
    STAMP(2);
    s16x4 ad[9][2][2];
    {
      LAS unsigned char* rb = L + L_W + (8 * g + q4) * ROWB;
#pragma unroll
      for (int u = 0; u < 8; ++u)
#pragma unroll
        for (int ks = 0; ks < 2; ++ks)
#pragma unroll
          for (int t = 0; t < 2; ++t)
            ad[u][ks][t] = trread(rb + (16 * w + 4 * p + ((u >> 1) ^ q4)) * 16 + 8 * (u & 1) + ks * 32 * ROWB + t * 4 * ROWB);
#pragma unroll
      for (int ks = 0; ks < 2; ++ks)
#pragma unroll
        for (int t = 0; t < 2; ++t)
          ad[8][ks][t] = trread(rb + ((64 + 2 * w + (p >> 1)) ^ q4) * 16 + 8 * (p & 1) + ks * 32 * ROWB + t * 4 * ROWB);
    }
    unsigned av, uv;
    for (unsigned spins = 0;; ++spins) {
      asm volatile("" ::: "memory");
      av = __builtin_amdgcn_raw_buffer_load_b32(rs, WS_AU + b * 512 + lane * 4, 0, AUX_SC1);
      uv = __builtin_amdgcn_raw_buffer_load_b32(rs, WS_AU + b * 512 + 256 + lane * 4, 0, AUX_SC1);
      if (__all((av != SENT) & (uv != SENT)) || spins > SPIN_CAP) break;
    }
    STAMP(3);
    {
      const float a0 = bflo(av), a1 = bfhi(av);
      const float h0 = bfr(a0 * sigmoidf_(a0)) * bflo(uv), h1 = bfr(a1 * sigmoidf_(a1)) * bfhi(uv);
      const unsigned hp = pack2(h0, h1);
      LAS unsigned short* hs = (LAS unsigned short*)(L + L_HS + w * 256);
      hs[lane] = (unsigned short)hp;
      hs[64 + lane] = (unsigned short)(hp >> 16);
    }
    const v4u hf0 = *(const LAS v4u*)(L + L_HS + w * 256 + (jp >> 3) * 128 + g * 16);
    const v4u hf1 = *(const LAS v4u*)(L + L_HS + w * 256 + (jp >> 3) * 128 + 64 + g * 16);
    f32x4 y[9];
#pragma unroll
    for (int u = 0; u < 9; ++u) {
      y[u] = __builtin_amdgcn_mfma_f32_16x16x32_bf16(frag(ad[u][0][0], ad[u][0][1]), frag(hf0), f32x4{0.f, 0.f, 0.f, 0.f}, 0, 0, 0);
      y[u] = __builtin_amdgcn_mfma_f32_16x16x32_bf16(frag(ad[u][1][0], ad[u][1][1]), frag(hf1), y[u], 0, 0, 0);
    }
    {
      const int t = jp >> 3, u = jp & 7;
      f32x4 sel = y[0];
#pragma unroll
      for (int uu = 1; uu < 8; ++uu) sel = (u == uu) ? y[uu] : sel;
      v4u o = __builtin_bit_cast(v4u, sel);
      o.x = min(o.x, SENT - 1u); o.y = min(o.y, SENT - 1u); o.z = min(o.z, SENT - 1u); o.w = min(o.w, SENT - 1u);
      __builtin_amdgcn_raw_buffer_store_b128(o, rs, WS_PART + ((18 * t + 4 * w + g) * NDN + b) * 128 + u * 16, 0, AUX_SC1);
      if (u == 0) {
        v4u o2 = __builtin_bit_cast(v4u, y[8]);
        o2.x = min(o2.x, SENT - 1u); o2.y = min(o2.y, SENT - 1u); o2.z = min(o2.z, SENT - 1u); o2.w = min(o2.w, SENT - 1u);
        __builtin_amdgcn_raw_buffer_store_b128(o2, rs, WS_PART + ((18 * t + 16 + (w >> 1)) * NDN + b) * 128 + (16 * (w & 1) + 4 * g) * 4, 0, AUX_SC1);
      }
    }
    STAMP(4);
    if (routes) {
      unsigned g0, g1;
      gather_top2(rs, tlo, thi, lane, c, dd, g0, g1);
      if (key != g0 && key != g1) {
        const __amdgpu_buffer_rsrc_t ro = __builtin_amdgcn_make_buffer_rsrc((void*)out, 0, S * ROWB, 0x00020000);
        const int ooff = row * ROWB + jj * 16;
#pragma unroll
        for (int i = 0; i < 9; ++i) {
          v4u ov;
#pragma unroll
          for (int e = 0; e < 4; ++e) {
            const float lo = bflo(xv[i][e]), hi = bfhi(xv[i][e]);
            ov[e] = pack2(__builtin_fmaf(lo, 0.0f, lo), __builtin_fmaf(hi, 0.0f, hi));
          }
          __builtin_amdgcn_raw_buffer_store_b128(ov, ro, ooff + i * 128, 0, MOE_OAUX);
        }
      }
    }
    STAMP(5);
  }
}
}

extern "C" void kernel_launch(void* const* d_in, const int* in_sizes, int n_in, void* d_out, int out_size, void* d_ws,
                              size_t ws_size, hipStream_t stream) {
  (void)in_sizes; (void)n_in; (void)out_size; (void)ws_size;
  (void)hipFuncSetAttribute((const void*)moe::moe_kernel, hipFuncAttributeMaxDynamicSharedMemorySize, moe::L_BYTES);
  moe::moe_kernel<<<moe::NWG, 256, moe::L_BYTES, stream>>>(
      (const unsigned char*)d_in[0], (const unsigned char*)d_in[1], (const unsigned char*)d_in[2], (const unsigned char*)d_in[3],
      (const unsigned char*)d_in[4], (unsigned char*)d_out, (unsigned char*)d_ws);
}
```

```cpp
#include <hip/hip_runtime.h>
#include <stdint.h>

#define GAS __attribute__((address_space(1)))
#define LAS __attribute__((address_space(3)))
typedef unsigned v4u __attribute__((ext_vector_type(4)));
typedef unsigned v2u __attribute__((ext_vector_type(2)));
typedef float f32x4 __attribute__((ext_vector_type(4)));
typedef __bf16 bf16x8 __attribute__((ext_vector_type(8)));
typedef __bf16 bf16x2 __attribute__((ext_vector_type(2)));
typedef short s16x4 __attribute__((ext_vector_type(4)));
typedef unsigned long long u64;

namespace moe {
constexpr int S = 2048, D = 576, F = 4608;
constexpr int NDN = 72;
constexpr int NSLOT = NDN + 36;
constexpr int RPW = 5;
constexpr int NGU = 144;
constexpr int NRED = 36;
constexpr int NWG = NDN + NGU + NRED;
constexpr int ROWB = D * 2;
constexpr int FROWB = F * 2;
constexpr int WS_EPOCH = 0;
constexpr int SLOT = 64;
constexpr int WS_CAND = 4096;
constexpr int WS_ARM = 12288;
constexpr int WS_ARM2 = 16384;
constexpr int WS_AU = 32768;
constexpr int AU_GRP = 2 * 3 * 512;
constexpr int WS_PART = 262144;
constexpr int WS_BYTES = WS_PART + NRED * NDN * 128;
constexpr int DPITCH = 1040, L_TAIL = 64 * DPITCH + 16;
constexpr int L_W = 0, L_X = L_TAIL + 8192, L_WR = L_X + 2432, L_KEY = L_WR + 4 * 1024, L_HS = L_KEY + 256, L_RED = L_HS + 1024, L_BYTES = 84 * 1024;
static_assert(L_RED + 512 <= L_BYTES, "LDS map");
static_assert(WS_CAND + NSLOT * SLOT <= WS_ARM && WS_ARM + NRED * SLOT <= WS_ARM2 && WS_ARM2 + NDN * SLOT <= 24576 &&
              WS_AU + NDN * AU_GRP <= WS_PART, "workspace map");
__device__ __forceinline__ int part_off(int rho, int prod) { return WS_PART + (rho * NDN + prod) * 128; }
constexpr unsigned SENT = 0xFFFFFFFFu;
constexpr unsigned SPIN_CAP = 1u << 19;
constexpr int AUX_SC1 = 16;

#define MOE_GU_AT 30
#define MOE_DN_AT 190

__device__ __forceinline__ float as_f(unsigned u) { return __builtin_bit_cast(float, u); }
__device__ __forceinline__ unsigned as_u(float f) { return __builtin_bit_cast(unsigned, f); }
__device__ __forceinline__ float dot2(unsigned a, unsigned b, float c) {
  return __builtin_amdgcn_fdot2_f32_bf16(__builtin_bit_cast(bf16x2, a), __builtin_bit_cast(bf16x2, b), c, false);
}
__device__ __forceinline__ float bflo(unsigned u) { return as_f(u << 16); }
__device__ __forceinline__ float bfhi(unsigned u) { return as_f(u & 0xffff0000u); }
__device__ __forceinline__ unsigned f2bf(float f) { return (unsigned)__builtin_bit_cast(unsigned short, (__bf16)f); }
__device__ __forceinline__ float bfr(float f) { return as_f(f2bf(f) << 16); }
__device__ __forceinline__ unsigned pack2(float lo, float hi) {
  bf16x2 v; v[0] = (__bf16)lo; v[1] = (__bf16)hi; return __builtin_bit_cast(unsigned, v);
}
__device__ __forceinline__ float sigmoidf_(float a) { return __builtin_amdgcn_rcpf(1.0f + __expf(-a)); }
__device__ __forceinline__ bf16x8 frag(s16x4 a, s16x4 b) {
  return __builtin_bit_cast(bf16x8, __builtin_shufflevector(a, b, 0, 1, 2, 3, 4, 5, 6, 7));
}
__device__ __forceinline__ bf16x8 frag(v4u a) { return __builtin_bit_cast(bf16x8, a); }
__device__ __forceinline__ s16x4 trread(LAS unsigned char* p) { return __builtin_amdgcn_ds_read_tr16_b64_v4i16((LAS s16x4*)p); }
__device__ __forceinline__ void glds16(const void* sbase, unsigned voff, unsigned lds) {
  unsigned keep;
  asm volatile("s_mov_b32 %0, m0\n\ts_mov_b32 m0, %3\n\ts_nop 0\n\tglobal_load_lds_dwordx4 %1, %2\n\ts_mov_b32 m0, %0"
               : "=&s"(keep) : "v"(voff), "s"(sbase), "s"(lds) : "memory");
}
__device__ __forceinline__ void glds16x(const void* sbase, unsigned voff, unsigned lds) {
  unsigned keep;
  asm volatile("s_mov_b32 %0, m0\n\ts_mov_b32 m0, %3\n\ts_nop 0\n\tglobal_load_lds_dwordx4 %1, %2 sc1\n\ts_mov_b32 m0, %0"
               : "=&s"(keep) : "v"(voff), "s"(sbase), "s"(lds) : "memory");
}
template <int N, int VSTEP, int LSTEP>
__device__ __forceinline__ void glds_batch(const void* sbase, unsigned voff, unsigned lds) {
  unsigned keep;
  asm volatile("s_mov_b32 %0, m0\n\ts_mov_b32 m0, %3\n\ts_nop 0\n\t.rept %c4\n\t"
               "global_load_lds_dwordx4 %1, %2 nt\n\ts_add_u32 m0, m0, %c6\n\tv_add_u32 %1, %c5, %1\n\t"
               ".endr\n\ts_mov_b32 m0, %0"
               : "=&s"(keep), "+v"(voff) : "s"(sbase), "s"(lds), "n"(N), "n"(VSTEP), "n"(LSTEP) : "memory", "scc");
}
constexpr int CODE_BYTES = 12544;
__device__ __forceinline__ void touch_code(u64 entry, int lane, unsigned lds) {
  unsigned keep;
  const unsigned v0 = (unsigned)lane * 128u, v1 = min(8192u + (unsigned)lane * 128u, (unsigned)CODE_BYTES - 128u);
  asm volatile("s_mov_b32 %0, m0\n\ts_mov_b32 m0, %4\n\ts_nop 0\n\tglobal_load_lds_dword %1, %3\n\tglobal_load_lds_dword %2, %3\n\ts_mov_b32 m0, %0"
               : "=&s"(keep) : "v"(v0), "v"(v1), "s"(entry), "s"(lds) : "memory");
}
template <class T> __device__ __forceinline__ void pin(T& v) { asm volatile("" : "+v"(v)); }
__device__ __forceinline__ u64 ballot(bool c) { return __builtin_amdgcn_ballot_w64(c); }
__device__ __forceinline__ unsigned max3u(unsigned a, unsigned b, unsigned c) { return max(max(a, b), c); }
__device__ __forceinline__ unsigned mkkey(unsigned rb, int row) {
  unsigned u = (rb & 0x7fffu) ? rb : 0u;
  unsigned m = (u & 0x8000u) ? (~u & 0xffffu) : (u | 0x8000u);
  m = ((u & 0x7fffu) > 0x7f80u) ? 0u : m;
  return (m << 11) | (unsigned)(2047 - row);
}
__device__ __forceinline__ int key_row(unsigned k) { return 2047 - (int)(k & 2047u); }
__device__ __forceinline__ float key_r(unsigned k) {
  unsigned m = k >> 11;
  unsigned rb = (m & 0x8000u) ? (m & 0x7fffu) : (m ? (~m & 0xffffu) : 0x7fc0u);
  return as_f(rb << 16);
}
template <int CTRL, int RM> __device__ __forceinline__ unsigned dppu(unsigned v) {
  return (unsigned)__builtin_amdgcn_update_dpp(0, (int)v, CTRL, RM, 0xf, true);
}
template <int CTRL, int RM> __device__ __forceinline__ float dppf(float v) { return as_f(dppu<CTRL, RM>(as_u(v))); }
template <int CTRL, int RM> __device__ __forceinline__ void top2_step(unsigned& k0, unsigned& k1) {
  const unsigned o0 = dppu<CTRL, RM>(k0), o1 = dppu<CTRL, RM>(k1);
  const unsigned lo = min(k0, o0);
  k0 = max(k0, o0);
  k1 = max(max(k1, o1), lo);
}
__device__ __forceinline__ void half_top2(unsigned& k0, unsigned& k1) {
  top2_step<0x111, 0xf>(k0, k1);
  top2_step<0x112, 0xf>(k0, k1);
  top2_step<0x114, 0xf>(k0, k1);
  top2_step<0x118, 0xf>(k0, k1);
  top2_step<0x142, 0xa>(k0, k1);
  k0 = (unsigned)__builtin_amdgcn_readlane((int)k0, 31);
  k1 = (unsigned)__builtin_amdgcn_readlane((int)k1, 31);
}
__device__ __forceinline__ void wave_top2(unsigned& k0, unsigned& k1) {
  top2_step<0x111, 0xf>(k0, k1);
  top2_step<0x112, 0xf>(k0, k1);
  top2_step<0x114, 0xf>(k0, k1);
  top2_step<0x118, 0xf>(k0, k1);
  top2_step<0x142, 0xa>(k0, k1);
  top2_step<0x143, 0xc>(k0, k1);
  k0 = (unsigned)__builtin_amdgcn_readlane((int)k0, 63);
  k1 = (unsigned)__builtin_amdgcn_readlane((int)k1, 63);
}
__device__ __forceinline__ v4u ld_slot_a(__amdgpu_buffer_rsrc_t rs, int lane) {
  return __builtin_amdgcn_raw_buffer_load_b128(rs, WS_CAND + lane * SLOT, 0, AUX_SC1);
}
__device__ __forceinline__ v4u ld_slot_b(__amdgpu_buffer_rsrc_t rs, int lane, int grp) {
  return __builtin_amdgcn_raw_buffer_load_b128(rs, lane == NSLOT - 64 ? WS_ARM2 + grp * SLOT : WS_CAND + (64 + (lane < NSLOT - 64 ? lane : 0)) * SLOT, 0, AUX_SC1);
}
__device__ __forceinline__ void gather_top2(__amdgpu_buffer_rsrc_t rs, unsigned tlo, unsigned thi, int lane, int grp, v4u c, v4u d,
                                            unsigned& k0, unsigned& k1) {
  for (unsigned n = SPIN_CAP;;) {
    const u64 bad = ballot(((c.x ^ tlo) | (c.z ^ tlo) | (d.x ^ tlo) | (d.z ^ tlo) | (((c.y ^ thi) | (c.w ^ thi) | (d.y ^ thi) | (d.w ^ thi)) & 31u)) != 0u);
    if (bad == 0ull || --n == 0u) break;
    asm volatile("" ::: "memory");
    c = ld_slot_a(rs, lane);
    d = ld_slot_b(rs, lane, grp);
  }
  k0 = c.y >> 5; k1 = c.w >> 5;
  const unsigned e0 = lane < NSLOT - 64 ? d.y >> 5 : 0u, e1 = lane < NSLOT - 64 ? d.w >> 5 : 0u;
  const unsigned lo = min(k0, e0);
  k0 = max(k0, e0);
  k1 = max(max(k1, e1), lo);
  wave_top2(k0, k1);
}

__global__ void __launch_bounds__(256, 2)
moe_kernel(const unsigned char* __restrict__ x, const unsigned char* __restrict__ wr, const unsigned char* __restrict__ wg,
           const unsigned char* __restrict__ wu, const unsigned char* __restrict__ wd, unsigned char* out, unsigned char* ws) {
  extern __shared__ __attribute__((aligned(16))) unsigned char smem[];
  LAS unsigned char* L = (LAS unsigned char*)smem;
  u64 entry;
  asm volatile("s_getpc_b64 %0" : "=s"(entry));
  entry &= ~255ull;
  const int tid = threadIdx.x, lane = tid & 63, w = __builtin_amdgcn_readfirstlane(tid >> 6), b = blockIdx.x;
  if (b >= NDN && b < NDN + 16 && w == 3) touch_code(entry, lane, (unsigned)(size_t)L + L_HS);
  asm volatile("" :: "s"(x), "s"(wr), "s"(wg), "s"(wu), "s"(wd), "s"(out), "s"(ws));
  const __amdgpu_buffer_rsrc_t rs = __builtin_amdgcn_make_buffer_rsrc((void*)ws, 0, WS_BYTES, 0x00020000);
  v2u ev;
  const int g = lane >> 4, q4 = (lane >> 2) & 3, p = lane & 3, jp = lane & 15;
  const unsigned lbase = (unsigned)(size_t)L;
  const u64 t_start = __builtin_amdgcn_s_memrealtime();
  const int slow0 = (3 - (int)((size_t)wg >> 7)) & 7;

  const bool isgu = b >= NDN && b < NDN + NGU;
  const bool isred = b >= NDN + NGU;
  const int gam = b - NDN, jg = gam >> 1, m = gam & 1;
  const bool slow = (jg & 7) == slow0;
  const int rho = isred ? b - (NDN + NGU) : 0;
  const int slot = isred ? NDN + rho : b;
  const int rw = lane >> 3, jj = lane & 7;
  const int row = slot * (4 * RPW) + w * RPW + rw;
  const bool act = rw < RPW && row < S;
  v4u xv[9], wtail;
  if (isgu) {
    ev = __builtin_amdgcn_raw_buffer_load_b64(rs, WS_EPOCH, 0, AUX_SC1);
    while (__builtin_amdgcn_s_memrealtime() - t_start < MOE_GU_AT) __builtin_amdgcn_s_sleep(1);
    const int s = (((lane >> 4) & 1) << 1) | ((w & 1) << 2);
    const unsigned voff = (unsigned)((8 * w + (lane >> 3)) * FROWB + ((lane & 7) ^ s) * 16);
    const unsigned char* W = (m ? wu : wg) + jg * 128;
    glds_batch<12, 32 * FROWB, 4096>(W, voff, lbase + L_W + w * 1024);
    if (!slow) glds_batch<6, 32 * FROWB, 4096>(W, voff + 12 * 32 * FROWB, lbase + L_W + w * 1024 + 12 * 4096);
  } else {
    glds16(wr, (unsigned)lane * 16, lbase + L_WR + w * 1024);
    const int lrow = min(slot * (4 * RPW) + w * RPW + min(rw, RPW - 1), S - 1);
    const GAS v4u* xp = (const GAS v4u*)((const GAS unsigned char*)x + (size_t)lrow * ROWB) + jj;
#pragma unroll
    for (int i = 0; i < 9; ++i) xv[i] = __builtin_nontemporal_load(xp + 8 * i);
    wtail = ((const GAS v4u*)wr)[64 + jj];
    ev = __builtin_amdgcn_raw_buffer_load_b64(rs, WS_EPOCH, 0, AUX_SC1);
  }
  int b2 = b;
  asm volatile("" : "+s"(b2));
  if (b2 >= NDN && b2 < NDN + NGU) {
    v4u c = {0u, 0u, 0u, 0u}, dd = {0u, 0u, 0u, 0u};
    if (w == 0) {
      asm volatile("s_waitcnt vmcnt(2)" ::: "memory");
      c = ld_slot_a(rs, lane); dd = ld_slot_b(rs, lane, jg);
      asm volatile("s_waitcnt vmcnt(2)" ::: "memory");
    } else {
      asm volatile("s_waitcnt vmcnt(0)" ::: "memory");
    }
    __builtin_amdgcn_s_barrier();
    if (w == 0) {
      const u64 tag = ((((u64)ev.y << 32) | ev.x) + 1ull);
      const unsigned tlo = __builtin_amdgcn_readfirstlane((unsigned)tag), thi = __builtin_amdgcn_readfirstlane((unsigned)(tag >> 32) & 31u);
      unsigned k0, k1;
      gather_top2(rs, tlo, thi, lane, jg, c, dd, k0, k1);
      const int r0 = key_row(k0), r1 = key_row(k1);
      const unsigned lx = lbase + L_X;
      glds16x(x + (size_t)r0 * ROWB, (unsigned)lane * 16, lx);
      glds16x(x + (size_t)r1 * ROWB, (unsigned)lane * 16, lx + 1088);
      if (lane < 16) glds16x(x, (unsigned)((lane < 8 ? r0 : r1) * ROWB + 1024 + (lane & 7) * 16), lx + 2176);
    }
    s16x4 a[18][2];
    {
      const int s = (((q4 >> 1) & 1) << 1) | ((g & 1) << 2);
      LAS unsigned char* base = L + L_W + (8 * g + q4) * 128 + (((2 * w + (p >> 1)) ^ s) * 16) + 8 * (p & 1);
#pragma unroll
      for (int ks = 0; ks < 18; ++ks) { a[ks][0] = trread(base + ks * 4096); a[ks][1] = trread(base + ks * 4096 + 512); }
    }
    int auo = WS_AU + jg * AU_GRP + m * 1536 + (16 * w + lane) * 8;
    unsigned xb = lbase + L_X + (jp & 1) * 1088 + g * 16, xt = lbase + L_X + 2176 + (jp & 1) * 128 + g * 16;
    pin(auo); pin(xb); pin(xt);
    asm volatile("s_waitcnt vmcnt(0) lgkmcnt(0)" ::: "memory");
    __builtin_amdgcn_s_barrier();
    f32x4 acc = {0.f, 0.f, 0.f, 0.f};
    {
      v4u xf[18];
#pragma unroll
      for (int ks = 0; ks < 12; ++ks) xf[ks] = *(const LAS v4u*)(size_t)(xb + ks * 64);
      __builtin_amdgcn_sched_barrier(0);
#pragma unroll
      for (int ks = 0; ks < 12; ++ks) {
        if (ks < 6) xf[ks + 12] = *(const LAS v4u*)(size_t)(ks < 4 ? xb + (ks + 12) * 64 : xt + (ks - 4) * 64);
        acc = __builtin_amdgcn_mfma_f32_16x16x32_bf16(frag(xf[ks]), frag(a[ks][0], a[ks][1]), acc, 0, 0, 0);
        __builtin_amdgcn_sched_barrier(0);
      }
      if (!slow) {
#pragma unroll
        for (int ks = 12; ks < 18; ++ks) acc = __builtin_amdgcn_mfma_f32_16x16x32_bf16(frag(xf[ks]), frag(a[ks][0], a[ks][1]), acc, 0, 0, 0);
      }
    }
    if (lane < 16) __builtin_amdgcn_raw_buffer_store_b64(v2u{as_u(acc[0]), as_u(acc[1])}, rs, auo, 0, AUX_SC1);
    return;
  }

  unsigned key = 0u;
  {
    if (!isred && w == 0) {
      const v4u s = {SENT, SENT, SENT, SENT};
      if ((b & 7) == slow0) {
#pragma unroll
        for (int i = 0; i < 3; ++i) __builtin_amdgcn_raw_buffer_store_b128(s, rs, WS_AU + b * AU_GRP + (lane + 64 * i) * 16, 0, AUX_SC1);
      } else {
        __builtin_amdgcn_raw_buffer_store_b128(s, rs, WS_AU + b * AU_GRP + (lane >> 5) * 1536 + (lane & 31) * 16, 0, AUX_SC1);
      }
    }
    __builtin_amdgcn_sched_barrier(0);
    asm volatile("s_waitcnt vmcnt(11)" ::: "memory");
    v4u wv[9];
#pragma unroll
    for (int i = 0; i < 8; ++i) wv[i] = *(const LAS v4u*)(L + L_WR + w * 1024 + (jj + 8 * i) * 16);
    wv[8] = wtail;
    float s0 = 0.f, s1 = 0.f, s2 = 0.f, s3 = 0.f;
#pragma unroll
    for (int i = 0; i < 9; ++i) {
      s0 = dot2(xv[i].x, wv[i].x, s0); s1 = dot2(xv[i].y, wv[i].y, s1);
      s2 = dot2(xv[i].z, wv[i].z, s2); s3 = dot2(xv[i].w, wv[i].w, s3);
    }
    float r = (s0 + s1) + (s2 + s3);
    r += dppf<0xB1, 0xf>(r);
    r += dppf<0x4E, 0xf>(r);
    r += dppf<0x141, 0xf>(r);
    key = act ? mkkey(f2bf(r), row) : 0u;
    LAS unsigned* keys = (LAS unsigned*)(L + L_KEY);
    if (jj == 0) keys[w * 8 + rw] = key;
  }
  asm volatile("s_waitcnt lgkmcnt(0)" ::: "memory");
  __builtin_amdgcn_s_barrier();
  const u64 tag = ((((u64)ev.y << 32) | ev.x) + 1ull);
  const unsigned tlo = __builtin_amdgcn_readfirstlane((unsigned)tag), thi = __builtin_amdgcn_readfirstlane((unsigned)(tag >> 32) & 31u);
  unsigned g0 = 0u, g1 = 0u;
  const int hj = 8 * (rho >> 2) + slow0, hm = (rho >> 1) & 1, hh = rho & 1;
  {
    LAS unsigned* keys = (LAS unsigned*)(L + L_KEY);
    if (w == 0) {
      unsigned k0 = keys[lane & 31], k1 = 0u;
      half_top2(k0, k1);
      if (lane == 0) {
        __builtin_amdgcn_raw_buffer_store_b128(v4u{tlo, (k0 << 5) | thi, tlo, (k1 << 5) | thi}, rs, WS_CAND + slot * SLOT, 0, AUX_SC1);
        keys[32] = k0; keys[33] = k1;
      }
      if (!isred) {
        asm volatile("s_waitcnt vmcnt(1)" ::: "memory");
        if (lane == 0) __builtin_amdgcn_raw_buffer_store_b128(v4u{tlo, thi, tlo, thi}, rs, WS_ARM2 + b * SLOT, 0, AUX_SC1);
      }
    }
    if (isred) {
      {
        const v4u s = {SENT, SENT, SENT, SENT};
#pragma unroll
        for (int i = 0; i < 3; ++i)
          if (i < 2 || w == 0) __builtin_amdgcn_raw_buffer_store_b128(s, rs, part_off(rho, 0) + (tid + 256 * i) * 16, 0, AUX_SC1);
      }
      const int s = (((lane >> 4) & 1) << 1) | ((w & 1) << 2);
      const unsigned voff = (unsigned)((384 + 96 * hh + 8 * w + (lane >> 3)) * FROWB + ((lane & 7) ^ s) * 16);
      glds_batch<3, 32 * FROWB, 4096>((hm ? wu : wg) + hj * 128, voff, lbase + L_W + w * 1024);
    }
    if (isred) {
      asm volatile("s_waitcnt vmcnt(3)" ::: "memory");
      __builtin_amdgcn_s_barrier();
      if (tid == 64) __builtin_amdgcn_raw_buffer_store_b64(v2u{tlo, thi}, rs, WS_ARM + rho * SLOT, 0, AUX_SC1);
      if (w == 0) {
        gather_top2(rs, tlo, thi, lane, hj, ld_slot_a(rs, lane), ld_slot_b(rs, lane, hj), g0, g1);
        if (lane < 24) {
          const int tk = lane >= 12, ch = lane - 12 * tk;
          glds16x(x, (unsigned)(key_row(tk ? g1 : g0) * ROWB + (384 + 96 * hh) * 2 + ch * 16), lbase + L_X);
        }
        if (lane == 0) { keys[0] = g0; keys[1] = g1; }
      }
      asm volatile("s_waitcnt vmcnt(0) lgkmcnt(0)" ::: "memory");
      __builtin_amdgcn_s_barrier();
      g0 = keys[0]; g1 = keys[1];
    }
  }
  auto residual = [&]() {
#pragma unroll
    for (int i = 0; i < 9; ++i)
#pragma unroll
      for (int e = 0; e < 4; ++e) {
        const float lo = bflo(xv[i][e]), hi = bfhi(xv[i][e]);
        xv[i][e] = pack2(__builtin_fmaf(lo, 0.0f, lo), __builtin_fmaf(hi, 0.0f, hi));
      }
  };
  auto copy_out = [&](bool plain, bool cands) {
    const __amdgpu_buffer_rsrc_t ro = __builtin_amdgcn_make_buffer_rsrc((void*)out, 0, S * ROWB, 0x00020000);
    const LAS unsigned* keys = (const LAS unsigned*)(L + L_KEY);
    const bool cand = key == keys[32] || key == keys[33];
    if (act && (cand ? (cands && key != g0 && key != g1) : plain)) {
#pragma unroll
      for (int i = 0; i < 9; ++i) __builtin_amdgcn_raw_buffer_store_b128(xv[i], ro, row * ROWB + jj * 16 + i * 128, 0, AUX_SC1);
    }
  };

  if (isred) {
    {
      const int s = (((q4 >> 1) & 1) << 1) | ((g & 1) << 2);
      LAS unsigned char* base = L + L_W + (8 * g + q4) * 128 + (((2 * w + (p >> 1)) ^ s) * 16) + 8 * (p & 1);
      LAS unsigned char* xb = L + L_X + (jp & 1) * 192 + g * 16;
      f32x4 acc = {0.f, 0.f, 0.f, 0.f};
#pragma unroll
      for (int ks = 0; ks < 3; ++ks)
        acc = __builtin_amdgcn_mfma_f32_16x16x32_bf16(frag(*(const LAS v4u*)(xb + ks * 64)), frag(trread(base + ks * 4096), trread(base + ks * 4096 + 512)), acc, 0, 0, 0);
      if (lane < 16)
        __builtin_amdgcn_raw_buffer_store_b64(v2u{as_u(acc[0]), as_u(acc[1])}, rs, WS_AU + hj * AU_GRP + hm * 1536 + (1 + hh) * 512 + (16 * w + lane) * 8, 0, AUX_SC1);
    }
    const int t = rho / 18, n0 = (rho % 18) * 32;
    const unsigned ksel = t ? g1 : g0;
    const int orow = key_row(ksel);
    float sig = 0.f, xr = 0.f;
    if (w == 0) {
      sig = bfr(sigmoidf_(key_r(ksel)));
      xr = bflo(*(const GAS unsigned short*)((const GAS unsigned char*)x + (size_t)orow * ROWB + (n0 + (lane & 31)) * 2));
    }
    residual();
    copy_out(true, true);
    const int ib = part_off(rho, 0) + tid * 16;
    unsigned rw_ = lbase + L_RED + (w * 32 + (lane & 7) * 4 + (((g & 1) << 1) | (g >> 1))) * 4, rr_ = lbase + L_RED + (lane & 31) * 4;
    int oo = orow * ROWB + (n0 + (lane & 31)) * 2;
    pin(rw_); pin(rr_); pin(oo);
    v4u v[3];
    for (unsigned n = SPIN_CAP;;) {
      asm volatile("" ::: "memory");
      v[0] = __builtin_amdgcn_raw_buffer_load_b128(rs, ib, 0, AUX_SC1);
      v[1] = __builtin_amdgcn_raw_buffer_load_b128(rs, ib + 4096, 0, AUX_SC1);
      v[2] = v4u{0u, 0u, 0u, 0u};
      if (w == 0) v[2] = __builtin_amdgcn_raw_buffer_load_b128(rs, ib + 8192, 0, AUX_SC1);
      const unsigned mx = max(max3u(max3u(v[0].x, v[0].y, v[0].z), max3u(v[0].w, v[1].x, v[1].y), max3u(v[1].z, v[1].w, v[2].x)), max3u(v[2].y, v[2].z, v[2].w));
      if (ballot(mx == SENT) == 0ull || --n == 0u) break;
    }
    const f32x4 acc = (__builtin_bit_cast(f32x4, v[0]) + __builtin_bit_cast(f32x4, v[1])) + __builtin_bit_cast(f32x4, v[2]);
    {
      const auto s0 = __builtin_amdgcn_permlane32_swap(as_u(acc[0]), as_u(acc[1]), false, false);
      const auto s1 = __builtin_amdgcn_permlane32_swap(as_u(acc[2]), as_u(acc[3]), false, false);
      const float p01 = as_f(s0[0]) + as_f(s0[1]);
      const float p23 = as_f(s1[0]) + as_f(s1[1]);
      const auto s2 = __builtin_amdgcn_permlane16_swap(as_u(p01), as_u(p23), false, false);
      float sum = as_f(s2[0]) + as_f(s2[1]);
      sum += dppf<0x128, 0xf>(sum);
      if ((lane & 8) == 0) *(LAS float*)(size_t)rw_ = sum;
    }
    asm volatile("s_waitcnt lgkmcnt(0)" ::: "memory");
    __builtin_amdgcn_s_barrier();
    if (w == 0) {
      const LAS float* red = (const LAS float*)(size_t)rr_;
      float y = (red[0] + red[32]) + (red[64] + red[96]);
      y = bfr(bfr(y) * sig);
      const float upd = bfr(__builtin_fmaf(xr, 0.0f, y));
      const unsigned ob = f2bf(xr + upd);
      if (lane < 32) *(GAS unsigned short*)((GAS unsigned char*)out + oo) = (unsigned short)ob;
      if (rho == 0 && lane == 0) {
        const u64 e1 = ((((u64)ev.y << 32) | ev.x) + 1ull);
        __builtin_amdgcn_raw_buffer_store_b64(v2u{(unsigned)e1, (unsigned)(e1 >> 32)}, rs, WS_EPOCH, 0, AUX_SC1);
      }
    }
    return;
  }

  {
    {
      while (__builtin_amdgcn_s_memrealtime() - t_start < MOE_DN_AT) __builtin_amdgcn_s_sleep(1);
      const unsigned char* d0 = wd + (size_t)(64 * b + 16 * w) * ROWB;
      glds_batch<8, ROWB, DPITCH>(d0, (unsigned)(lane * 16), lbase + L_W + 16 * w * DPITCH);
      glds_batch<8, ROWB, DPITCH>(d0, (unsigned)(lane * 16 + 8 * ROWB), lbase + L_W + (16 * w + 8) * DPITCH + 8);
      glds_batch<2, 8 * ROWB, 1024>(d0, (unsigned)((lane >> 3) * ROWB + 1024 + (lane & 7) * 16), lbase + L_TAIL + w * 2048);
    }
    v2u arm = __builtin_amdgcn_raw_buffer_load_b64(rs, WS_ARM + (lane < NRED ? lane : 0) * SLOT, 0, AUX_SC1);
    const v4u sa = ld_slot_a(rs, lane), sb = ld_slot_b(rs, lane, b);
    __builtin_amdgcn_sched_barrier(0);
    residual();
#pragma unroll
    for (int i = 0; i < 9; ++i) pin(xv[i]);
    int po0 = part_off(18 * (jp >> 3) + 4 * w + g, b) + (jp & 7) * 16;
    int po1 = part_off(18 * (jp >> 3) + 16 + (w >> 1), b) + (16 * (w & 1) + 4 * g) * 4;
    int auo = WS_AU + b * AU_GRP + lane * 8;
    unsigned hw = lbase + L_HS + w * 256 + lane * 2, hr = lbase + L_HS + w * 256 + (jp >> 3) * 128 + g * 16;
    pin(po0); pin(po1); pin(auo); pin(hw); pin(hr);
    const bool slowg = (b & 7) == slow0;
    asm volatile("s_waitcnt vmcnt(0)" ::: "memory");
    __builtin_amdgcn_s_barrier();
    for (unsigned n = SPIN_CAP;;) {
      if (ballot(((arm.x ^ tlo) | ((arm.y ^ thi) & 31u)) != 0u) == 0ull || --n == 0u) break;
      asm volatile("" ::: "memory");
      arm = __builtin_amdgcn_raw_buffer_load_b64(rs, WS_ARM + (lane < NRED ? lane : 0) * SLOT, 0, AUX_SC1);
    }
    v2u v[6];
#pragma unroll
    for (int i = 0; i < 6; ++i) v[i] = v2u{0u, 0u};
    v[0] = __builtin_amdgcn_raw_buffer_load_b64(rs, auo, 0, AUX_SC1);
    v[3] = __builtin_amdgcn_raw_buffer_load_b64(rs, auo + 1536, 0, AUX_SC1);
    if (slowg) {
      v[1] = __builtin_amdgcn_raw_buffer_load_b64(rs, auo + 512, 0, AUX_SC1);
      v[2] = __builtin_amdgcn_raw_buffer_load_b64(rs, auo + 1024, 0, AUX_SC1);
      v[4] = __builtin_amdgcn_raw_buffer_load_b64(rs, auo + 2048, 0, AUX_SC1);
      v[5] = __builtin_amdgcn_raw_buffer_load_b64(rs, auo + 2560, 0, AUX_SC1);
    }
    __builtin_amdgcn_sched_barrier(0);
    s16x4 ad[9][2][2];
    {
      LAS unsigned char* rb = L + L_W + (8 * g + q4) * DPITCH + 8 * (g & 1) + (16 * w + 4 * p) * 16;
#pragma unroll
      for (int u = 0; u < 8; ++u)
#pragma unroll
        for (int ks = 0; ks < 2; ++ks)
#pragma unroll
          for (int t = 0; t < 2; ++t)
            ad[u][ks][t] = trread(rb + u * 8 + ks * 32 * DPITCH + t * 4 * DPITCH);
      LAS unsigned char* tb = L + L_TAIL + (8 * g + q4) * 128 + w * 32 + p * 8;
#pragma unroll
      for (int ks = 0; ks < 2; ++ks)
#pragma unroll
        for (int t = 0; t < 2; ++t) ad[8][ks][t] = trread(tb + ks * 32 * 128 + t * 4 * 128);
    }
    __builtin_amdgcn_sched_barrier(0);
    for (unsigned n = SPIN_CAP;;) {
      const unsigned mx = max(max3u(max3u(v[0].x, v[0].y, v[3].x), max3u(v[3].y, v[1].x, v[1].y), max3u(v[2].x, v[2].y, v[4].x)), max3u(v[4].y, v[5].x, v[5].y));
      if (ballot(mx == SENT) == 0ull || --n == 0u) break;
      asm volatile("" ::: "memory");
      v[0] = __builtin_amdgcn_raw_buffer_load_b64(rs, auo, 0, AUX_SC1);
      v[3] = __builtin_amdgcn_raw_buffer_load_b64(rs, auo + 1536, 0, AUX_SC1);
      if (slowg) {
        v[1] = __builtin_amdgcn_raw_buffer_load_b64(rs, auo + 512, 0, AUX_SC1);
        v[2] = __builtin_amdgcn_raw_buffer_load_b64(rs, auo + 1024, 0, AUX_SC1);
        v[4] = __builtin_amdgcn_raw_buffer_load_b64(rs, auo + 2048, 0, AUX_SC1);
        v[5] = __builtin_amdgcn_raw_buffer_load_b64(rs, auo + 2560, 0, AUX_SC1);
      }
    }
    float a0 = (as_f(v[0].x) + as_f(v[1].x)) + as_f(v[2].x), a1 = (as_f(v[0].y) + as_f(v[1].y)) + as_f(v[2].y);
    const float u0 = (as_f(v[3].x) + as_f(v[4].x)) + as_f(v[5].x), u1 = (as_f(v[3].y) + as_f(v[4].y)) + as_f(v[5].y);
    {
      a0 = bfr(a0); a1 = bfr(a1);
      const float h0 = bfr(a0 * sigmoidf_(a0)) * bfr(u0), h1 = bfr(a1 * sigmoidf_(a1)) * bfr(u1);
      const unsigned hp = pack2(h0, h1);
      LAS unsigned short* hs = (LAS unsigned short*)(size_t)hw;
      hs[0] = (unsigned short)hp;
      hs[64] = (unsigned short)(hp >> 16);
    }
    const v4u hf0 = *(const LAS v4u*)(size_t)hr;
    const v4u hf1 = *(const LAS v4u*)(size_t)(hr + 64);
    f32x4 y[9];
#pragma unroll
    for (int u = 0; u < 9; ++u) {
      y[u] = __builtin_amdgcn_mfma_f32_16x16x32_bf16(frag(ad[u][0][0], ad[u][0][1]), frag(hf0), f32x4{0.f, 0.f, 0.f, 0.f}, 0, 0, 0);
      y[u] = __builtin_amdgcn_mfma_f32_16x16x32_bf16(frag(ad[u][1][0], ad[u][1][1]), frag(hf1), y[u], 0, 0, 0);
    }
    {
      f32x4 sel = y[0];
#pragma unroll
      for (int uu = 1; uu < 8; ++uu) sel = __builtin_amdgcn_inverse_ballot_w64(0x0101010101010101ull << uu) ? y[uu] : sel;
      __builtin_amdgcn_raw_buffer_store_b128(__builtin_bit_cast(v4u, sel), rs, po0, 0, AUX_SC1);
      if ((jp & 7) == 0) __builtin_amdgcn_raw_buffer_store_b128(__builtin_bit_cast(v4u, y[8]), rs, po1, 0, AUX_SC1);
    }
    copy_out(true, false);
    gather_top2(rs, tlo, thi, lane, b, sa, sb, g0, g1);
    copy_out(false, true);
  }
}
}

extern "C" void kernel_launch(void* const* d_in, const int* in_sizes, int n_in, void* d_out, int out_size, void* d_ws,
                              size_t ws_size, hipStream_t stream) {
  (void)in_sizes; (void)n_in; (void)out_size; (void)ws_size;
  (void)hipFuncSetAttribute((const void*)moe::moe_kernel, hipFuncAttributeMaxDynamicSharedMemorySize, moe::L_BYTES);
  moe::moe_kernel<<<moe::NWG, 256, moe::L_BYTES, stream>>>(
      (const unsigned char*)d_in[0], (const unsigned char*)d_in[1], (const unsigned char*)d_in[2], (const unsigned char*)d_in[3],
      (const unsigned char*)d_in[4], (unsigned char*)d_out, (unsigned char*)d_ws);
}
```
